```python
import torch

def _softmax(x, axis=-1):
    return torch.softmax(x, dim=axis)
def _gelu(x):
    return torch.nn.functional.gelu(x, approximate="tanh")
def _silu(x):
    return torch.nn.functional.silu(x)
def _sigmoid(x):
    return torch.sigmoid(x)
def _layernorm(x, axis=-1, eps=1e-5):
    m = x.mean(dim=axis, keepdim=True)
    v = x.var(dim=axis, keepdim=True, unbiased=False)
    return (x - m) / torch.sqrt(v + eps)
def _scatter_add(tgt, idx, src, axis=0):
    return tgt.clone().index_add_(axis, idx, src)
def _segment_sum(src, seg, n):
    out = torch.zeros((n,) + src.shape[1:], dtype=src.dtype)
    return out.index_add_(0, seg, src)
def _pad(x, pw):
    flat = tuple(v for pair in reversed(pw) for v in pair)
    return torch.nn.functional.pad(x, flat)

kernel_name = 'recurrent_scan__sliding_block__glu_gated_geglu__prompt'

def setup_inputs():

    _w = (lambda a: (lambda t: t.to(torch.bfloat16) if t.is_floating_point() else t)(torch.as_tensor(a)))
    def _A_setup():
        B, S, D = 1, 2048, 512
        return {
            "x": _w((lambda *s: torch.randn(*s, dtype=torch.bfloat16) * 0.05)(B, S, D)),
            "wqkv": _w((lambda *s: torch.randn(*s, dtype=torch.bfloat16) * 0.2)(D, 3 * D)),
            "wo": _w((lambda *s: torch.randn(*s, dtype=torch.bfloat16) * 0.2)(D, D)),
        }
    _A_in = _A_setup()

    def _B_setup():
        B, S, Dm, Df = 1, 2048, 512, 1792
        return {
            "x": _w((lambda *s: torch.randn(*s, dtype=torch.bfloat16) * 0.05)(B, S, Dm)),
            "w_gate": _w((lambda *s: torch.randn(*s, dtype=torch.bfloat16) * 0.1)(Dm, Df) * (Dm ** -0.5)),
            "w_up": _w((lambda *s: torch.randn(*s, dtype=torch.bfloat16) * 0.1)(Dm, Df) * (Dm ** -0.5)),
            "w_down": _w((lambda *s: torch.randn(*s, dtype=torch.bfloat16) * 0.1)(Df, Dm) * (Df ** -0.5)),
        }
    _B_in = _B_setup()

    return {
        "A_x": _A_in["x"],
        "A_wqkv": _A_in["wqkv"],
        "A_wo": _A_in["wo"],
        "B_x": _B_in["x"],
        "B_w_gate": _B_in["w_gate"],
        "B_w_up": _B_in["w_up"],
        "B_w_down": _B_in["w_down"],
    }

def reference(A_x, A_wqkv, A_wo, B_x, B_w_gate, B_w_up, B_w_down):
    def _A_ref(x, wqkv, wo):
        B, S, D = x.shape
        H = 32; Dh = D // H; W = 256
        qkv = (lambda x, *d: x.permute(*d))((x @ wqkv).reshape(B, S, 3, H, Dh), 2, 0, 3, 1, 4)
        q, k, v = qkv[0], qkv[1], qkv[2]
        scores = torch.einsum("bhqd,bhkd->bhqk", q, k) * (Dh ** -0.5)
        idx = torch.arange(S)
        mask = (idx[:, None] - idx[None, :] >= 0) & (idx[:, None] - idx[None, :] < W)
        scores = torch.where(mask[None, None], scores, -1e9)
        p = _softmax(scores, axis=-1)
        o = torch.einsum("bhqk,bhkd->bhqd", p, v)
        return (lambda x, *d: x.permute(*d))(o, 0, 2, 1, 3).reshape(B, S, D) @ wo

    def _B_ref(x, w_gate, w_up, w_down):
        g = torch.einsum("bsd,df->bsf", x, w_gate)
        u = torch.einsum("bsd,df->bsf", x, w_up)
        h = _gelu(g) * u
        return torch.einsum("bsf,fd->bsd", h, w_down)

    x = A_x
    xn = _layernorm(x, axis=-1)
    s = _A_ref(xn, A_wqkv, A_wo)
    g = _silu(_B_ref(xn, B_w_gate, B_w_up, B_w_down))
    return x + s * g
```

```cpp
#include <hip/hip_runtime.h>
#include <hip/hip_bf16.h>

typedef unsigned short u16;
typedef short bf16x8 __attribute__((ext_vector_type(8)));
typedef short bf16x4 __attribute__((ext_vector_type(4)));
typedef float f32x4 __attribute__((ext_vector_type(4)));
typedef float f32x16 __attribute__((ext_vector_type(16)));
typedef __attribute__((address_space(3))) bf16x4* lds_bf16x4_ptr;

#define DEV __device__ __forceinline__

constexpr int S_ = 2048, D_ = 512, NQKV = 1536, DF = 1792, H_ = 32, DH = 16;

DEV float bf2f(u16 v) { return __uint_as_float(((unsigned)v) << 16); }
DEV unsigned pack2(float lo, float hi) {
  __hip_bfloat16 a = __float2bfloat16(lo), b = __float2bfloat16(hi);
  return (unsigned)*reinterpret_cast<u16*>(&a) | ((unsigned)*reinterpret_cast<u16*>(&b) << 16);
}
DEV bf16x4 tr_read(const char* lds_base, unsigned off) {
  return __builtin_amdgcn_ds_read_tr16_b64_v4i16((lds_bf16x4_ptr)(lds_base + off));
}
DEV bf16x8 cat8(bf16x4 a, bf16x4 b) {
  bf16x8 r;
  r[0] = a[0]; r[1] = a[1]; r[2] = a[2]; r[3] = a[3];
  r[4] = b[0]; r[5] = b[1]; r[6] = b[2]; r[7] = b[3];
  return r;
}
DEV float gelu_tanh(float g) {
  const float c0 = 0.7978845608028654f, c1 = 0.044715f;
  float y = c0 * (g + c1 * g * g * g);
  float e = __expf(2.f * y);
  float t = 1.f - 2.f / (e + 1.f);
  return 0.5f * g * (1.f + t);
}
DEV float silu(float v) { return v / (1.f + __expf(-v)); }

constexpr int K1_THREADS = 512;
constexpr int K1_A_STAGE = 128 * 64 * 2;
constexpr int K1_B_STAGE = 64 * 320 * 2;
constexpr int K1_LDS = 2 * (K1_A_STAGE + K1_B_STAGE);

__global__ __launch_bounds__(K1_THREADS, 2) void k1_ln_gemm(
    const u16* __restrict__ x, const u16* __restrict__ wqkv, const u16* __restrict__ wg, const u16* __restrict__ wu,
    u16* __restrict__ Qo, u16* __restrict__ Ko, u16* __restrict__ Vto, u16* __restrict__ ho) {
  __shared__ __attribute__((aligned(16))) char lds[K1_LDS];
  char* As = lds;
  char* Bs = lds + 2 * K1_A_STAGE;

  const int tid = threadIdx.x, lane = tid & 63, wid = tid >> 6;
  const int wm = wid >> 2, wn = wid & 3;
  const int bid = blockIdx.x;
  const int xcd = bid & 7, idx = bid >> 3;
  const int mt = (xcd >> 1) * 4 + (idx & 3);
  const int nt = (xcd & 1) * 8 + (idx >> 2);
  const int m0 = mt * 128;
  const bool mixed = nt < 14;

  const u16* bsrc[5]; int bld[5];
  if (mixed) {
    bsrc[0] = wqkv + nt * 64;           bld[0] = NQKV;
    bsrc[1] = wg + nt * 128;            bld[1] = DF;
    bsrc[2] = wg + nt * 128 + 64;       bld[2] = DF;
    bsrc[3] = wu + nt * 128;            bld[3] = DF;
    bsrc[4] = wu + nt * 128 + 64;       bld[4] = DF;
  } else {
#pragma unroll
    for (int j = 0; j < 5; ++j) { bsrc[j] = wqkv + 896 + (nt - 14) * 320 + j * 64; bld[j] = NQKV; }
  }
  {
    const int k = tid >> 3, pc = tid & 7;
    const int fk = ((k >> 1) & 1) | (((k >> 3) & 1) << 1);
    const int lc = pc ^ (fk << 1);
#pragma unroll
    for (int j = 0; j < 5; ++j) bsrc[j] += (long)k * bld[j] + lc * 8;
  }
  auto stageB = [&](int stage, int s) {
#pragma unroll
    for (int j = 0; j < 5; ++j) {
      __builtin_amdgcn_global_load_lds((const unsigned*)(bsrc[j] + (long)s * 64 * bld[j]),
                                       (unsigned*)(Bs + stage * K1_B_STAGE + j * 8192 + tid * 16), 16, 0, 0);
    }
  };
  stageB(0, 0);

  uint4 xr[8][2];
  {
    const int r = tid >> 2, j = tid & 3;
    const u16* xp = x + (long)(m0 + r) * D_ + j * 16;
#pragma unroll
    for (int s = 0; s < 8; ++s) {
      xr[s][0] = *reinterpret_cast<const uint4*>(xp + s * 64);
      xr[s][1] = *reinterpret_cast<const uint4*>(xp + s * 64 + 8);
    }
    float sum = 0.f;
#pragma unroll
    for (int s = 0; s < 8; ++s)
#pragma unroll
      for (int q = 0; q < 2; ++q) {
        const unsigned* w = reinterpret_cast<const unsigned*>(&xr[s][q]);
#pragma unroll
        for (int e = 0; e < 4; ++e) { sum += __uint_as_float(w[e] << 16); sum += __uint_as_float(w[e] & 0xffff0000u); }
      }
    sum += __shfl_xor(sum, 1); sum += __shfl_xor(sum, 2);
    const float mean = sum * (1.f / D_);
    float vs = 0.f;
#pragma unroll
    for (int s = 0; s < 8; ++s)
#pragma unroll
      for (int q = 0; q < 2; ++q) {
        const unsigned* w = reinterpret_cast<const unsigned*>(&xr[s][q]);
#pragma unroll
        for (int e = 0; e < 4; ++e) {
          float a = __uint_as_float(w[e] << 16) - mean, b = __uint_as_float(w[e] & 0xffff0000u) - mean;
          vs += a * a; vs += b * b;
        }
      }
    vs += __shfl_xor(vs, 1); vs += __shfl_xor(vs, 2);
    const float rstd = 1.f / sqrtf(vs * (1.f / D_) + 1e-5f);
#pragma unroll
    for (int s = 0; s < 8; ++s)
#pragma unroll
      for (int q = 0; q < 2; ++q) {
        unsigned* w = reinterpret_cast<unsigned*>(&xr[s][q]);
#pragma unroll
        for (int e = 0; e < 4; ++e) {
          float a = (__uint_as_float(w[e] << 16) - mean) * rstd, b = (__uint_as_float(w[e] & 0xffff0000u) - mean) * rstd;
          w[e] = pack2(a, b);
        }
      }
  }
  auto stageA = [&](int stage, int s) {
    const int r = tid >> 2, j = tid & 3;
    char* dst = As + stage * K1_A_STAGE + r * 128;
    *reinterpret_cast<uint4*>(dst + (((2 * j) ^ (r & 7)) << 4)) = xr[s][0];
    *reinterpret_cast<uint4*>(dst + (((2 * j + 1) ^ (r & 7)) << 4)) = xr[s][1];
  };
  stageA(0, 0);

  const int arow = wm * 64 + (lane & 15);
  const int g = lane >> 4, q = (lane & 15) >> 2, p = lane & 3;
  const int fk = (q >> 1) | ((g & 1) << 1);
  unsigned boff[5];
  int cfs[5];
#pragma unroll
  for (int f = 0; f < 5; ++f) {
    int cf;
    if (mixed) cf = (f == 0) ? 16 * wn : (f <= 2 ? 64 + 32 * wn + 16 * (f - 1) : 192 + 32 * wn + 16 * (f - 3));
    else cf = 80 * wn + 16 * f;
    cfs[f] = cf;
    boff[f] = (cf >> 6) * 8192 + (8 * g + q) * 128 + ((((cf & 63) >> 4) ^ fk) << 5) + p * 8;
  }

  f32x4 acc[4][5];
#pragma unroll
  for (int m = 0; m < 4; ++m)
#pragma unroll
    for (int f = 0; f < 5; ++f) acc[m][f] = f32x4{0.f, 0.f, 0.f, 0.f};

  for (int s = 0; s < 8; ++s) {
    __syncthreads();
    if (s + 1 < 8) { stageB((s + 1) & 1, s + 1); stageA((s + 1) & 1, s + 1); }
    const char* Ab = As + (s & 1) * K1_A_STAGE;
    const char* Bb = Bs + (s & 1) * K1_B_STAGE;
#pragma unroll
    for (int ks = 0; ks < 2; ++ks) {
      bf16x8 af[4], bfr[5];
#pragma unroll
      for (int m = 0; m < 4; ++m) {
        const int r = arow + 16 * m;
        af[m] = *reinterpret_cast<const bf16x8*>(Ab + r * 128 + ((((lane >> 4) + 4 * ks) ^ (r & 7)) << 4));
      }
#pragma unroll
      for (int f = 0; f < 5; ++f) {
        bf16x4 lo = tr_read(Bb, boff[f] + (32 * ks) * 128);
        bf16x4 hi = tr_read(Bb, boff[f] + (32 * ks + 4) * 128);
        bfr[f] = cat8(lo, hi);
      }
#pragma unroll
      for (int m = 0; m < 4; ++m)
#pragma unroll
        for (int f = 0; f < 5; ++f)
          acc[m][f] = __builtin_amdgcn_mfma_f32_16x16x32_bf16(bfr[f], af[m], acc[m][f], 0, 0, 0);
    }
  }

  const int n4 = (lane >> 4) * 4;
  auto store_qkv = [&](int col, int tok, const f32x4& v) {
    const int which = col >> 9, h = (col & 511) >> 4;
    uint2 pk; pk.x = pack2(v[0], v[1]); pk.y = pack2(v[2], v[3]);
    if (which < 2) {
      u16* dst = (which == 0 ? Qo : Ko) + ((long)h * S_ + tok) * 16 + n4;
      *reinterpret_cast<uint2*>(dst) = pk;
    } else {
      u16* dst = Vto + ((long)h * 16 + n4) * S_ + tok;
      dst[0] = (u16)(pk.x & 0xffff); dst[S_] = (u16)(pk.x >> 16);
      dst[2 * S_] = (u16)(pk.y & 0xffff); dst[3 * S_] = (u16)(pk.y >> 16);
    }
  };
#pragma unroll
  for (int m = 0; m < 4; ++m) {
    const int tok = m0 + wm * 64 + 16 * m + (lane & 15);
    if (mixed) {
      store_qkv(nt * 64 + cfs[0], tok, acc[m][0]);
#pragma unroll
      for (int f = 0; f < 2; ++f) {
        const f32x4 gg = acc[m][1 + f], uu = acc[m][3 + f];
        float hv[4];
#pragma unroll
        for (int j = 0; j < 4; ++j) hv[j] = gelu_tanh(gg[j]) * uu[j];
        uint2 pk; pk.x = pack2(hv[0], hv[1]); pk.y = pack2(hv[2], hv[3]);
        const int col = nt * 128 + 32 * wn + 16 * f + n4;
        *reinterpret_cast<uint2*>(ho + (long)tok * DF + col) = pk;
      }
    } else {
#pragma unroll
      for (int f = 0; f < 5; ++f) store_qkv(896 + (nt - 14) * 320 + cfs[f], tok, acc[m][f]);
    }
  }
}

constexpr int G_STAGE = 8192;
template <int KTOT>
DEV void gemm64(const u16* __restrict__ A, int lda, const u16* __restrict__ B, int ldb, int m0, int n0,
                char* lds, f32x4 (&acc)[2][2]) {
  char* As = lds;
  char* Bs = lds + 2 * G_STAGE;
  const int tid = threadIdx.x, lane = tid & 63, wid = tid >> 6;
  const int wm = wid >> 1, wn = wid & 1;
  const u16* asrc; const u16* bsrc;
  {
    const int r = tid >> 3, pc = tid & 7;
    asrc = A + (long)(m0 + r) * lda + ((pc ^ (r & 7)) * 8);
    const int fk = ((r >> 1) & 1) | (((r >> 3) & 1) << 1);
    bsrc = B + (long)r * ldb + n0 + ((pc ^ (fk << 1)) * 8);
  }
  auto stage = [&](int st, int s) {
    __builtin_amdgcn_global_load_lds((const unsigned*)(asrc + s * 64), (unsigned*)(As + st * G_STAGE + tid * 16), 16, 0, 0);
    __builtin_amdgcn_global_load_lds((const unsigned*)(asrc + (long)32 * lda + s * 64), (unsigned*)(As + st * G_STAGE + 4096 + tid * 16), 16, 0, 0);
    __builtin_amdgcn_global_load_lds((const unsigned*)(bsrc + (long)s * 64 * ldb), (unsigned*)(Bs + st * G_STAGE + tid * 16), 16, 0, 0);
    __builtin_amdgcn_global_load_lds((const unsigned*)(bsrc + (long)(s * 64 + 32) * ldb), (unsigned*)(Bs + st * G_STAGE + 4096 + tid * 16), 16, 0, 0);
  };
  const int g = lane >> 4, q = (lane & 15) >> 2, p = lane & 3;
  const int fk = (q >> 1) | ((g & 1) << 1);
  constexpr int NS = KTOT / 64;
  stage(0, 0);
  for (int s = 0; s < NS; ++s) {
    __syncthreads();
    if (s + 1 < NS) stage((s + 1) & 1, s + 1);
    const char* Ab = As + (s & 1) * G_STAGE;
    const char* Bb = Bs + (s & 1) * G_STAGE;
#pragma unroll
    for (int ks = 0; ks < 2; ++ks) {
      bf16x8 af[2], bfr[2];
#pragma unroll
      for (int m = 0; m < 2; ++m) {
        const int r = wm * 32 + 16 * m + (lane & 15);
        af[m] = *reinterpret_cast<const bf16x8*>(Ab + r * 128 + ((((lane >> 4) + 4 * ks) ^ (r & 7)) << 4));
      }
#pragma unroll
      for (int n = 0; n < 2; ++n) {
        const unsigned off = (8 * g + q + 32 * ks) * 128 + ((((wn * 2 + n)) ^ fk) << 5) + p * 8;
        bfr[n] = cat8(tr_read(Bb, off), tr_read(Bb, off + 4 * 128));
      }
#pragma unroll
      for (int m = 0; m < 2; ++m)
#pragma unroll
        for (int n = 0; n < 2; ++n)
          acc[m][n] = __builtin_amdgcn_mfma_f32_16x16x32_bf16(bfr[n], af[m], acc[m][n], 0, 0, 0);
    }
  }
}

constexpr int NB_DOWN = (S_ / 64) * (D_ / 64);
constexpr int NB_ATTN = H_ * (S_ / 32) / 4;

DEV void attn_wave(const u16* __restrict__ Q, const u16* __restrict__ Kk, const u16* __restrict__ Vt,
                   u16* __restrict__ o, int h, int q0, int lane) {
  const int r = lane & 31, hh = lane >> 5;
  const bf16x8 qf = *reinterpret_cast<const bf16x8*>(Q + ((long)h * S_ + q0 + r) * 16 + 8 * hh);
  f32x16 st[9];
  const float NEG = -3.0e38f;
#pragma unroll
  for (int t = 0; t < 9; ++t) {
    const int key0 = q0 - 256 + 32 * t;
    if (key0 >= 0) {
      const bf16x8 kf = *reinterpret_cast<const bf16x8*>(Kk + ((long)h * S_ + key0 + r) * 16 + 8 * hh);
      f32x16 z;
#pragma unroll
      for (int i = 0; i < 16; ++i) z[i] = 0.f;
      st[t] = __builtin_amdgcn_mfma_f32_32x32x16_bf16(kf, qf, z, 0, 0, 0);
    } else {
#pragma unroll
      for (int i = 0; i < 16; ++i) st[t][i] = NEG;
    }
  }
#pragma unroll
  for (int i = 0; i < 16; ++i) {
    const int kl = (i & 3) + 8 * (i >> 2) + 4 * hh;
    if (kl <= r) st[0][i] = NEG;
    if (kl > r) st[8][i] = NEG;
  }
  float mx = NEG;
#pragma unroll
  for (int t = 0; t < 9; ++t)
#pragma unroll
    for (int i = 0; i < 16; ++i) mx = fmaxf(mx, st[t][i]);
  mx = fmaxf(mx, __shfl_xor(mx, 32));
  const float c = 0.25f * 1.4426950408889634f;
  const float mc = mx * c;
  float sum = 0.f;
  f32x16 oacc;
#pragma unroll
  for (int i = 0; i < 16; ++i) oacc[i] = 0.f;
  const int dh = r & 15;
#pragma unroll
  for (int t = 0; t < 9; ++t) {
    const int key0 = q0 - 256 + 32 * t;
    float pv[16];
#pragma unroll
    for (int i = 0; i < 16; ++i) { pv[i] = exp2f(st[t][i] * c - mc); sum += pv[i]; }
    if (key0 >= 0) {
#pragma unroll
      for (int ss = 0; ss < 2; ++ss) {
        bf16x8 pf;
#pragma unroll
        for (int e = 0; e < 4; ++e) {
          unsigned w = pack2(pv[8 * ss + 2 * e], pv[8 * ss + 2 * e + 1]);
          pf[2 * e] = (short)(w & 0xffff); pf[2 * e + 1] = (short)(w >> 16);
        }
        const u16* vp = Vt + ((long)h * 16 + dh) * S_ + key0 + 16 * ss + 4 * hh;
        const bf16x4 v0 = *reinterpret_cast<const bf16x4*>(vp);
        const bf16x4 v1 = *reinterpret_cast<const bf16x4*>(vp + 8);
        oacc = __builtin_amdgcn_mfma_f32_32x32x16_bf16(cat8(v0, v1), pf, oacc, 0, 0, 0);
      }
    }
  }
  sum += __shfl_xor(sum, 32);
  const float inv = 1.f / sum;
  u16* op = o + (long)(q0 + r) * D_ + h * 16 + 4 * hh;
  uint2 a, b;
  a.x = pack2(oacc[0] * inv, oacc[1] * inv); a.y = pack2(oacc[2] * inv, oacc[3] * inv);
  b.x = pack2(oacc[4] * inv, oacc[5] * inv); b.y = pack2(oacc[6] * inv, oacc[7] * inv);
  *reinterpret_cast<uint2*>(op) = a;
  *reinterpret_cast<uint2*>(op + 8) = b;
}

__global__ __launch_bounds__(256, 2) void k2_attn_down(
    const u16* __restrict__ Q, const u16* __restrict__ Kk, const u16* __restrict__ Vt, u16* __restrict__ o,
    const u16* __restrict__ hbuf, const u16* __restrict__ wd, float* __restrict__ bout) {
  __shared__ __attribute__((aligned(16))) char lds[4 * G_STAGE];
  const int bid = blockIdx.x, tid = threadIdx.x, lane = tid & 63, wid = tid >> 6;
  if (bid < NB_DOWN) {
    const int mt = bid >> 3, nt = bid & 7;
    f32x4 acc[2][2];
#pragma unroll
    for (int m = 0; m < 2; ++m)
#pragma unroll
      for (int n = 0; n < 2; ++n) acc[m][n] = f32x4{0.f, 0.f, 0.f, 0.f};
    gemm64<DF>(hbuf, DF, wd, D_, mt * 64, nt * 64, lds, acc);
    const int wm = wid >> 1, wn = wid & 1;
#pragma unroll
    for (int m = 0; m < 2; ++m)
#pragma unroll
      for (int n = 0; n < 2; ++n) {
        const int tok = mt * 64 + wm * 32 + 16 * m + (lane & 15);
        const int col = nt * 64 + wn * 32 + 16 * n + (lane >> 4) * 4;
        *reinterpret_cast<f32x4*>(bout + (long)tok * D_ + col) = acc[m][n];
      }
  } else {
    const int task = (bid - NB_DOWN) * 4 + wid;
    const int h = task >> 6, qt = task & 63;
    attn_wave(Q, Kk, Vt, o, h, qt * 32, lane);
  }
}

__global__ __launch_bounds__(256, 2) void k3_wo_final(
    const u16* __restrict__ o, const u16* __restrict__ wo, const float* __restrict__ bout,
    const u16* __restrict__ x, u16* __restrict__ out) {
  __shared__ __attribute__((aligned(16))) char lds[4 * G_STAGE];
  const int bid = blockIdx.x, tid = threadIdx.x, lane = tid & 63, wid = tid >> 6;
  const int mt = bid >> 3, nt = bid & 7;
  f32x4 acc[2][2];
#pragma unroll
  for (int m = 0; m < 2; ++m)
#pragma unroll
    for (int n = 0; n < 2; ++n) acc[m][n] = f32x4{0.f, 0.f, 0.f, 0.f};
  gemm64<D_>(o, D_, wo, D_, mt * 64, nt * 64, lds, acc);
  const int wm = wid >> 1, wn = wid & 1;
#pragma unroll
  for (int m = 0; m < 2; ++m)
#pragma unroll
    for (int n = 0; n < 2; ++n) {
      const int tok = mt * 64 + wm * 32 + 16 * m + (lane & 15);
      const int col = nt * 64 + wn * 32 + 16 * n + (lane >> 4) * 4;
      const f32x4 bv = *reinterpret_cast<const f32x4*>(bout + (long)tok * D_ + col);
      const uint2 xv = *reinterpret_cast<const uint2*>(x + (long)tok * D_ + col);
      float r0 = __uint_as_float(xv.x << 16) + acc[m][n][0] * silu(bv[0]);
      float r1 = __uint_as_float(xv.x & 0xffff0000u) + acc[m][n][1] * silu(bv[1]);
      float r2 = __uint_as_float(xv.y << 16) + acc[m][n][2] * silu(bv[2]);
      float r3 = __uint_as_float(xv.y & 0xffff0000u) + acc[m][n][3] * silu(bv[3]);
      uint2 pk; pk.x = pack2(r0, r1); pk.y = pack2(r2, r3);
      *reinterpret_cast<uint2*>(out + (long)tok * D_ + col) = pk;
    }
}

extern "C" void kernel_launch(void* const* d_in, const int* in_sizes, int n_in,
                              void* d_out, int out_size, void* d_ws, size_t ws_size,
                              hipStream_t stream) {
  const u16* x = (const u16*)d_in[0];
  const u16* wqkv = (const u16*)d_in[1];
  const u16* wo = (const u16*)d_in[2];
  const u16* wg = (const u16*)d_in[4];
  const u16* wu = (const u16*)d_in[5];
  const u16* wd = (const u16*)d_in[6];
  u16* out = (u16*)d_out;
  char* ws = (char*)d_ws;
  u16* Qb = (u16*)(ws);
  u16* Kb = (u16*)(ws + (size_t)2 * 1024 * 1024);
  u16* Vb = (u16*)(ws + (size_t)4 * 1024 * 1024);
  u16* ob = (u16*)(ws + (size_t)6 * 1024 * 1024);
  float* bb = (float*)(ws + (size_t)8 * 1024 * 1024);
  u16* hb = (u16*)(ws + (size_t)12 * 1024 * 1024);
  k1_ln_gemm<<<256, K1_THREADS, 0, stream>>>(x, wqkv, wg, wu, Qb, Kb, Vb, hb);
  k2_attn_down<<<NB_DOWN + NB_ATTN, 256, 0, stream>>>(Qb, Kb, Vb, ob, hb, wd, bb);
  k3_wo_final<<<256, 256, 0, stream>>>(ob, wo, bb, x, out);
}
```

```cpp
#include <hip/hip_runtime.h>
#include <hip/hip_bf16.h>

typedef unsigned short u16;
typedef short bf16x8 __attribute__((ext_vector_type(8)));
typedef short bf16x4 __attribute__((ext_vector_type(4)));
typedef float f32x4 __attribute__((ext_vector_type(4)));
typedef float f32x16 __attribute__((ext_vector_type(16)));
typedef __attribute__((address_space(3))) bf16x4* lds_bf16x4_ptr;

#define DEV __device__ __forceinline__
#define STAMP(i)
#define STAMP_PARAM
#define STAMP_ARG(p)

constexpr int S_ = 2048, D_ = 512, NQKV = 1536, DF = 1792, H_ = 32, DH = 16;

DEV float bf2f(u16 v) { return __uint_as_float(((unsigned)v) << 16); }
typedef __bf16 bf16x2_n __attribute__((ext_vector_type(2)));
typedef float f32x2_n __attribute__((ext_vector_type(2)));
DEV unsigned pack2(float lo, float hi) {
  f32x2_n f = {lo, hi};
  bf16x2_n r = __builtin_convertvector(f, bf16x2_n);
  return *reinterpret_cast<unsigned*>(&r);
}
DEV bf16x4 tr_read(const char* lds_base, unsigned off) {
  return __builtin_amdgcn_ds_read_tr16_b64_v4i16((lds_bf16x4_ptr)(lds_base + off));
}
DEV void glds16(const void* sbase, unsigned voff, unsigned lds_dst) {
  unsigned keep;
  asm volatile("s_mov_b32 %0, m0\n\ts_mov_b32 m0, %3\n\ts_nop 0\n\tglobal_load_lds_dwordx4 %1, %2\n\ts_mov_b32 m0, %0"
               : "=&s"(keep) : "v"(voff), "s"(sbase), "s"(lds_dst) : "memory");
}
DEV unsigned lds_addr(const void* p) { return (unsigned)(size_t)p; }
DEV bf16x8 cat8(bf16x4 a, bf16x4 b) {
  bf16x8 r;
  r[0] = a[0]; r[1] = a[1]; r[2] = a[2]; r[3] = a[3];
  r[4] = b[0]; r[5] = b[1]; r[6] = b[2]; r[7] = b[3];
  return r;
}
DEV float dot2bf(unsigned a, unsigned b, float c) {
  typedef short s16x2 __attribute__((ext_vector_type(2)));
  return __builtin_amdgcn_fdot2_f32_bf16(*reinterpret_cast<s16x2*>(&a), *reinterpret_cast<s16x2*>(&b), c, false);
}
DEV float max3(float a, float b, float c) {
  float r; asm("v_max3_f32 %0, %1, %2, %3" : "=v"(r) : "v"(a), "v"(b), "v"(c)); return r;
}
DEV float gelu_tanh(float g) {
  const float c0 = 2.f * 0.7978845608028654f * 1.4426950408889634f, c1 = 0.044715f;
  const float y = c0 * g * fmaf(c1 * g, g, 1.f);
  return g * __builtin_amdgcn_rcpf(1.f + __builtin_amdgcn_exp2f(-y));
}
DEV float silu(float v) { return v * __builtin_amdgcn_rcpf(1.f + __builtin_amdgcn_exp2f(-1.4426950408889634f * v)); }

constexpr int K1_THREADS = 512;
constexpr int K1_A_STAGE = 128 * 64 * 2;
constexpr int K1_B_STAGE = 64 * 320 * 2;
constexpr int K1_LDS = 2 * K1_A_STAGE + 3 * K1_B_STAGE;

__global__ __launch_bounds__(K1_THREADS, 2) void k1_ln_gemm(
    const u16* __restrict__ x, const u16* __restrict__ wqkv, const u16* __restrict__ wg, const u16* __restrict__ wu,
    u16* __restrict__ Qo, u16* __restrict__ Ko, u16* __restrict__ Vto, u16* __restrict__ ho STAMP_PARAM) {
  __shared__ __attribute__((aligned(16))) char lds[K1_LDS];
  STAMP(0);
  char* As = lds;
  char* Bs = lds + 2 * K1_A_STAGE;

  const int tid = threadIdx.x, lane = tid & 63, wid = tid >> 6;
  const int wm = wid >> 2, wn = wid & 3;
  const int bid = blockIdx.x;
  const int xcd = bid & 7, idx = bid >> 3;
  const int mt = (xcd >> 1) * 4 + (idx & 3);
  const int nt = (xcd & 1) * 8 + (idx >> 2);
  const int m0 = mt * 128;
  const bool mixed = nt < 14;

  const u16* bsrc[5]; int bld[5];
  if (mixed) {
    bsrc[0] = wqkv + nt * 64;           bld[0] = NQKV;
    bsrc[1] = wg + nt * 128;            bld[1] = DF;
    bsrc[2] = wg + nt * 128 + 64;       bld[2] = DF;
    bsrc[3] = wu + nt * 128;            bld[3] = DF;
    bsrc[4] = wu + nt * 128 + 64;       bld[4] = DF;
  } else {
#pragma unroll
    for (int j = 0; j < 5; ++j) { bsrc[j] = wqkv + 896 + (nt - 14) * 320 + j * 64; bld[j] = NQKV; }
  }
  const int wave_lds = __builtin_amdgcn_readfirstlane(wid) * 1024;
  unsigned loff_q, loff_f;
  {
    const int k = tid >> 3, pc = tid & 7;
    const int fk = ((k >> 1) & 1) | (((k >> 3) & 1) << 1);
    const int lc = pc ^ (fk << 1);
    loff_q = (unsigned)(k * NQKV + lc * 8) * 2u;
    loff_f = (unsigned)(k * DF + lc * 8) * 2u;
  }
  const unsigned bs_lds = lds_addr(Bs) + wave_lds;
  auto stageB = [&](int stage, int s) {
#pragma unroll
    for (int j = 0; j < 5; ++j) {
      const unsigned lo = (bld[j] == NQKV) ? loff_q : loff_f;
      glds16(bsrc[j] + (long)s * 64 * bld[j], lo, bs_lds + stage * K1_B_STAGE + j * 8192);
    }
  };

  uint4 xr[8][2];
  {
    const int r = tid >> 2, j = tid & 3;
    const u16* xp = x + (long)(m0 + r) * D_ + j * 16;
#pragma unroll
    for (int s = 0; s < 8; ++s) {
      xr[s][0] = *reinterpret_cast<const uint4*>(xp + s * 64);
      xr[s][1] = *reinterpret_cast<const uint4*>(xp + s * 64 + 8);
    }
    asm volatile("" ::: "memory");
    stageB(0, 0);
    stageB(1, 1);
    float sum = 0.f;
#pragma unroll
    for (int s = 0; s < 8; ++s)
#pragma unroll
      for (int q = 0; q < 2; ++q) {
        const unsigned* w = reinterpret_cast<const unsigned*>(&xr[s][q]);
#pragma unroll
        for (int e = 0; e < 4; ++e) sum = dot2bf(w[e], 0x3F803F80u, sum);
      }
    sum += __shfl_xor(sum, 1); sum += __shfl_xor(sum, 2);
    STAMP(1);
    const float mean = sum * (1.f / D_);
    f32x2_n vs2 = {0.f, 0.f};
    const f32x2_n mean2 = {mean, mean};
#pragma unroll
    for (int s = 0; s < 8; ++s)
#pragma unroll
      for (int q = 0; q < 2; ++q) {
        const unsigned* w = reinterpret_cast<const unsigned*>(&xr[s][q]);
#pragma unroll
        for (int e = 0; e < 4; ++e) {
          f32x2_n d = {__uint_as_float(w[e] << 16), __uint_as_float(w[e] & 0xffff0000u)};
          d = d - mean2;
          vs2 = d * d + vs2;
        }
      }
    float vs = vs2[0] + vs2[1];
    vs += __shfl_xor(vs, 1); vs += __shfl_xor(vs, 2);
    const float rstd = 1.f / sqrtf(vs * (1.f / D_) + 1e-5f);
    const float nb = -mean * rstd;
#pragma unroll
    for (int s = 0; s < 8; ++s)
#pragma unroll
      for (int q = 0; q < 2; ++q) {
        unsigned* w = reinterpret_cast<unsigned*>(&xr[s][q]);
#pragma unroll
        for (int e = 0; e < 4; ++e) {
          w[e] = pack2(fmaf(__uint_as_float(w[e] << 16), rstd, nb), fmaf(__uint_as_float(w[e] & 0xffff0000u), rstd, nb));
        }
        asm volatile("" : "+v"(w[0]), "+v"(w[1]), "+v"(w[2]), "+v"(w[3]));
      }
  }
  auto stageA = [&](int stage, int s) {
    const int r = tid >> 2, j = tid & 3;
    char* dst = As + stage * K1_A_STAGE + r * 128;
    *reinterpret_cast<uint4*>(dst + (((2 * j) ^ (r & 7)) << 4)) = xr[s][0];
    *reinterpret_cast<uint4*>(dst + (((2 * j + 1) ^ (r & 7)) << 4)) = xr[s][1];
  };
  stageA(0, 0);
  STAMP(2);

  const int arow = wm * 64 + (lane & 15);
  const int g = lane >> 4, q = (lane & 15) >> 2, p = lane & 3;
  const int fk = (q >> 1) | ((g & 1) << 1);
  unsigned boff[5];
#pragma unroll
  for (int f = 0; f < 5; ++f) {
    int cf;
    if (mixed) cf = (f == 0) ? 16 * wn : (f <= 2 ? 64 + 32 * wn + 16 * (f - 1) : 192 + 32 * wn + 16 * (f - 3));
    else cf = 80 * wn + 16 * f;
    boff[f] = (cf >> 6) * 8192 + (8 * g + q) * 128 + ((((cf & 63) >> 4) ^ fk) << 5) + p * 8;
  }

  f32x4 acc[4][5];
#pragma unroll
  for (int m = 0; m < 4; ++m)
#pragma unroll
    for (int f = 0; f < 5; ++f) acc[m][f] = f32x4{0.f, 0.f, 0.f, 0.f};

#pragma unroll
  for (int s = 0; s < 8; ++s) {
    if (s < 7) asm volatile("s_waitcnt vmcnt(5) lgkmcnt(0)" ::: "memory");
    else asm volatile("s_waitcnt vmcnt(0) lgkmcnt(0)" ::: "memory");
    __builtin_amdgcn_s_barrier();
    asm volatile("" ::: "memory");
    STAMP(3 + s);
    if (s + 1 < 8) stageA((s + 1) & 1, s + 1);
    if (s + 2 < 8) stageB((s + 2) % 3, s + 2);
    int a_off = (s & 1) * K1_A_STAGE, b_off = (s % 3) * K1_B_STAGE;
    const char* Ab = As + a_off;
    const char* Bb = Bs + b_off;
#pragma unroll
    for (int ks = 0; ks < 2; ++ks) {
      bf16x8 af[4], bfr[5];
#pragma unroll
      for (int m = 0; m < 4; ++m) {
        const int r = arow + 16 * m;
        af[m] = *reinterpret_cast<const bf16x8*>(Ab + r * 128 + ((((lane >> 4) + 4 * ks) ^ (r & 7)) << 4));
      }
#pragma unroll
      for (int f = 0; f < 5; ++f) {
        bf16x4 lo = tr_read(Bb, boff[f] + (32 * ks) * 128);
        bf16x4 hi = tr_read(Bb, boff[f] + (32 * ks + 4) * 128);
        bfr[f] = cat8(lo, hi);
      }
#pragma unroll
      for (int m = 0; m < 4; ++m)
#pragma unroll
        for (int f = 0; f < 5; ++f)
          acc[m][f] = __builtin_amdgcn_mfma_f32_16x16x32_bf16(bfr[f], af[m], acc[m][f], 0, 0, 0);
    }
  }
  __syncthreads();
  STAMP(11);

  int tz = threadIdx.x; asm volatile("" : "+v"(tz));
  const int ln = tz & 63, wd_ = tz >> 6, wm2 = wd_ >> 2, wn2 = wd_ & 3;
  const int n4 = (ln >> 4) * 4;
  auto store_qkv = [&](int col, int tok, const f32x4& v) {
    const int which = col >> 9, h = (col & 511) >> 4;
    uint2 pk; pk.x = pack2(v[0], v[1]); pk.y = pack2(v[2], v[3]);
    if (which < 2) {
      u16* dst = (which == 0 ? Qo : Ko) + ((long)h * S_ + tok) * 16 + n4;
      *reinterpret_cast<uint2*>(dst) = pk;
    } else {
      char* tp = lds + wd_ * 512;
      *reinterpret_cast<uint2*>(tp + (ln & 15) * 32 + n4 * 2) = pk;
      asm volatile("s_waitcnt lgkmcnt(0)" ::: "memory");
      bf16x4 tv = tr_read(tp, ((ln >> 4) * 4 + ((ln & 15) >> 2)) * 32 + (ln & 3) * 8);
      asm volatile("" ::: "memory");
      const int tk0 = tok - (ln & 15), g4 = ln >> 4;
      u16* dst = Vto + (long)h * (S_ * 16) + tk0 * 16 + (g4 & 1) * 128 + (ln & 15) * 8 + (g4 >> 1) * 4;
      *reinterpret_cast<bf16x4*>(dst) = tv;
    }
  };
#pragma unroll
  for (int m = 0; m < 4; ++m) {
    const int tok = m0 + wm2 * 64 + 16 * m + (ln & 15);
    if (mixed) {
      store_qkv(nt * 64 + 16 * wn2, tok, acc[m][0]);
#pragma unroll
      for (int f = 0; f < 2; ++f) {
        const f32x4 gg = acc[m][1 + f], uu = acc[m][3 + f];
        float hv[4];
#pragma unroll
        for (int j = 0; j < 4; ++j) hv[j] = gelu_tanh(gg[j]) * uu[j];
        uint2 pk; pk.x = pack2(hv[0], hv[1]); pk.y = pack2(hv[2], hv[3]);
        const int col = nt * 128 + 32 * wn2 + 16 * f + n4;
        *reinterpret_cast<uint2*>(ho + (long)tok * DF + col) = pk;
      }
    } else {
#pragma unroll
      for (int f = 0; f < 5; ++f) store_qkv(896 + (nt - 14) * 320 + 80 * wn2 + 16 * f, tok, acc[m][f]);
    }
  }
  STAMP(12);
}

constexpr int G_STAGE = 8192;
template <int KTOT>
DEV void gemm64(const u16* __restrict__ A, int lda, const u16* __restrict__ B, int ldb, int m0, int n0,
                char* lds, f32x4 (&acc)[2][2]) {
  char* As = lds;
  char* Bs = lds + 4 * G_STAGE;
  const int tid = threadIdx.x, lane = tid & 63, wid = tid >> 6;
  const int wm = wid >> 1, wn = wid & 1;
  unsigned aoff, boff;
  {
    const int r = tid >> 3, pc = tid & 7;
    aoff = (unsigned)(r * lda + ((pc ^ (r & 7)) * 8)) * 2u;
    const int fk = ((r >> 1) & 1) | (((r >> 3) & 1) << 1);
    boff = (unsigned)(r * ldb + ((pc ^ (fk << 1)) * 8)) * 2u;
  }
  const u16* abase = A + (long)m0 * lda;
  const u16* bbase = B + n0;
  const int wave_lds = __builtin_amdgcn_readfirstlane(wid) * 1024;
  const unsigned as_lds = lds_addr(As) + wave_lds, bs_lds = lds_addr(Bs) + wave_lds;
  auto stage = [&](int st, int s) {
    glds16(abase + s * 64, aoff, as_lds + st * G_STAGE);
    glds16(abase + (long)32 * lda + s * 64, aoff, as_lds + st * G_STAGE + 4096);
    glds16(bbase + (long)s * 64 * ldb, boff, bs_lds + st * G_STAGE);
    glds16(bbase + (long)(s * 64 + 32) * ldb, boff, bs_lds + st * G_STAGE + 4096);
  };
  const int g = lane >> 4, q = (lane & 15) >> 2, p = lane & 3;
  const int fk = (q >> 1) | ((g & 1) << 1);
  constexpr int NS = KTOT / 64;
  stage(0, 0); stage(1, 1); stage(2, 2);
  for (int s = 0; s < NS; ++s) {
    if (s + 2 < NS) asm volatile("s_waitcnt vmcnt(8)" ::: "memory");
    else if (s + 1 < NS) asm volatile("s_waitcnt vmcnt(4)" ::: "memory");
    else asm volatile("s_waitcnt vmcnt(0)" ::: "memory");
    __builtin_amdgcn_s_barrier();
    asm volatile("" ::: "memory");
    if (s + 3 < NS) stage((s + 3) & 3, s + 3);
    const char* Ab = As + (s & 3) * G_STAGE;
    const char* Bb = Bs + (s & 3) * G_STAGE;
#pragma unroll
    for (int ks = 0; ks < 2; ++ks) {
      bf16x8 af[2], bfr[2];
#pragma unroll
      for (int m = 0; m < 2; ++m) {
        const int r = wm * 32 + 16 * m + (lane & 15);
        af[m] = *reinterpret_cast<const bf16x8*>(Ab + r * 128 + ((((lane >> 4) + 4 * ks) ^ (r & 7)) << 4));
      }
#pragma unroll
      for (int n = 0; n < 2; ++n) {
        const unsigned off = (8 * g + q + 32 * ks) * 128 + ((((wn * 2 + n)) ^ fk) << 5) + p * 8;
        bfr[n] = cat8(tr_read(Bb, off), tr_read(Bb, off + 4 * 128));
      }
#pragma unroll
      for (int m = 0; m < 2; ++m)
#pragma unroll
        for (int n = 0; n < 2; ++n)
          acc[m][n] = __builtin_amdgcn_mfma_f32_16x16x32_bf16(bfr[n], af[m], acc[m][n], 0, 0, 0);
    }
  }
}

constexpr int NB_DOWN = (S_ / 64) * (D_ / 64);
constexpr int NB_ATTN = H_ * (S_ / 32) / 4;

DEV void attn_wave(const u16* __restrict__ Q, const u16* __restrict__ Kk, const u16* __restrict__ Vt,
                   u16* __restrict__ o, int h, int q0, int lane STAMP_PARAM) {
  const int r = lane & 31, hh = lane >> 5;
  const bf16x8 qf = *reinterpret_cast<const bf16x8*>(Q + ((long)h * S_ + q0 + r) * 16 + 8 * hh);
  bf16x8 kf[9];
#pragma unroll
  for (int t = 0; t < 9; ++t) {
    const int key0 = max(q0 - 256 + 32 * t, 0);
    kf[t] = *reinterpret_cast<const bf16x8*>(Kk + ((long)h * S_ + key0 + r) * 16 + 8 * hh);
  }
  bf16x8 vf[9][2];
  {
    const short one = (short)0x3F80;
    bf16x8 ones8;
#pragma unroll
    for (int i = 0; i < 8; ++i) ones8[i] = one;
    const bool isone = (r >= 16);
    const u16* vbase = Vt + (long)h * (S_ * 16) + hh * 128 + (r & 15) * 8;
#pragma unroll
    for (int t = 0; t < 9; ++t) {
      const int key0 = max(q0 - 256 + 32 * t, 0);
#pragma unroll
      for (int ss = 0; ss < 2; ++ss) {
        const bf16x8 v = *reinterpret_cast<const bf16x8*>(vbase + (key0 + 16 * ss) * 16);
        vf[t][ss] = isone ? ones8 : v;
      }
    }
  }
  f32x16 st[9];
  const float NEG = -3.0e38f;
#pragma unroll
  for (int t = 0; t < 9; ++t) {
    f32x16 z;
#pragma unroll
    for (int i = 0; i < 16; ++i) z[i] = 0.f;
    st[t] = __builtin_amdgcn_mfma_f32_32x32x16_bf16(kf[t], qf, z, 0, 0, 0);
  }
  asm volatile("" :: "v"(st[8][0]));
  STAMP(3);
#pragma unroll
  for (int i = 0; i < 16; ++i) {
    const int kl = (i & 3) + 8 * (i >> 2) + 4 * hh;
    if (kl <= r) st[0][i] = NEG;
    if (kl > r) st[8][i] = NEG;
  }
  if (q0 < 256) {
#pragma unroll
    for (int t = 0; t < 8; ++t)
      if (q0 - 256 + 32 * t < 0) {
#pragma unroll
        for (int i = 0; i < 16; ++i) st[t][i] = NEG;
      }
  }
  float mx = NEG;
#pragma unroll
  for (int t = 0; t < 9; ++t)
#pragma unroll
    for (int i = 0; i < 16; i += 2) mx = max3(mx, st[t][i], st[t][i + 1]);
  mx = fmaxf(mx, __shfl_xor(mx, 32));
  STAMP(4);
  const float c = 0.25f * 1.4426950408889634f;
  const float mc = mx * c;
  f32x16 oacc;
#pragma unroll
  for (int i = 0; i < 16; ++i) oacc[i] = 0.f;
#pragma unroll
  for (int t = 0; t < 9; ++t) {
#pragma unroll
    for (int ss = 0; ss < 2; ++ss) {
      bf16x8 pf;
#pragma unroll
      for (int e = 0; e < 4; ++e) {
        const float p0 = __builtin_amdgcn_exp2f(st[t][8 * ss + 2 * e] * c - mc);
        const float p1 = __builtin_amdgcn_exp2f(st[t][8 * ss + 2 * e + 1] * c - mc);
        const unsigned w = pack2(p0, p1);
        pf[2 * e] = (short)(w & 0xffff); pf[2 * e + 1] = (short)(w >> 16);
      }
      oacc = __builtin_amdgcn_mfma_f32_32x32x16_bf16(vf[t][ss], pf, oacc, 0, 0, 0);
    }
  }
  STAMP(5);
  const float inv = 1.f / oacc[8];
  u16* op = o + (long)(q0 + r) * D_ + h * 16 + 4 * hh;
  uint2 a, b;
  a.x = pack2(oacc[0] * inv, oacc[1] * inv); a.y = pack2(oacc[2] * inv, oacc[3] * inv);
  b.x = pack2(oacc[4] * inv, oacc[5] * inv); b.y = pack2(oacc[6] * inv, oacc[7] * inv);
  *reinterpret_cast<uint2*>(op) = a;
  *reinterpret_cast<uint2*>(op + 8) = b;
}

__global__ __launch_bounds__(256, 2) void k2_attn_down(
    const u16* __restrict__ Q, const u16* __restrict__ Kk, const u16* __restrict__ Vt, u16* __restrict__ o,
    const u16* __restrict__ hbuf, const u16* __restrict__ wd, float* __restrict__ bout STAMP_PARAM) {
  __shared__ __attribute__((aligned(16))) char lds[8 * G_STAGE];
  const int bid = blockIdx.x, tid = threadIdx.x, lane = tid & 63, wid = tid >> 6;
  STAMP(0);
  if (bid < NB_DOWN) {
    const int mt = (bid & 7) * 4 + ((bid >> 3) & 3), nt = bid >> 5;
    f32x4 acc[2][2];
#pragma unroll
    for (int m = 0; m < 2; ++m)
#pragma unroll
      for (int n = 0; n < 2; ++n) acc[m][n] = f32x4{0.f, 0.f, 0.f, 0.f};
    gemm64<DF>(hbuf, DF, wd, D_, mt * 64, nt * 64, lds, acc);
    STAMP(1);
    const int wm = wid >> 1, wn = wid & 1;
#pragma unroll
    for (int m = 0; m < 2; ++m)
#pragma unroll
      for (int n = 0; n < 2; ++n) {
        const int tok = mt * 64 + wm * 32 + 16 * m + (lane & 15);
        const int col = nt * 64 + wn * 32 + 16 * n + (lane >> 4) * 4;
        *reinterpret_cast<f32x4*>(bout + (long)tok * D_ + col) = acc[m][n];
      }
  } else {
    const int task = (bid - NB_DOWN) * 4 + __builtin_amdgcn_readfirstlane(wid);
    const int h = task >> 6, qt = task & 63;
    attn_wave(Q, Kk, Vt, o, h, qt * 32, lane);
    STAMP(1);
  }
  STAMP(2);
}

__global__ __launch_bounds__(256, 2) void k3_wo_final(
    const u16* __restrict__ o, const u16* __restrict__ wo, const float* __restrict__ bout,
    const u16* __restrict__ x, u16* __restrict__ out) {
  __shared__ __attribute__((aligned(16))) char lds[8 * G_STAGE];
  const int bid = blockIdx.x, tid = threadIdx.x, lane = tid & 63, wid = tid >> 6;
  const int mt = (bid & 7) * 4 + ((bid >> 3) & 3), nt = bid >> 5;
  f32x4 acc[2][2];
#pragma unroll
  for (int m = 0; m < 2; ++m)
#pragma unroll
    for (int n = 0; n < 2; ++n) acc[m][n] = f32x4{0.f, 0.f, 0.f, 0.f};
  gemm64<D_>(o, D_, wo, D_, mt * 64, nt * 64, lds, acc);
  const int wm = wid >> 1, wn = wid & 1;
#pragma unroll
  for (int m = 0; m < 2; ++m)
#pragma unroll
    for (int n = 0; n < 2; ++n) {
      const int tok = mt * 64 + wm * 32 + 16 * m + (lane & 15);
      const int col = nt * 64 + wn * 32 + 16 * n + (lane >> 4) * 4;
      const f32x4 bv = *reinterpret_cast<const f32x4*>(bout + (long)tok * D_ + col);
      const uint2 xv = *reinterpret_cast<const uint2*>(x + (long)tok * D_ + col);
      float r0 = __uint_as_float(xv.x << 16) + acc[m][n][0] * silu(bv[0]);
      float r1 = __uint_as_float(xv.x & 0xffff0000u) + acc[m][n][1] * silu(bv[1]);
      float r2 = __uint_as_float(xv.y << 16) + acc[m][n][2] * silu(bv[2]);
      float r3 = __uint_as_float(xv.y & 0xffff0000u) + acc[m][n][3] * silu(bv[3]);
      uint2 pk; pk.x = pack2(r0, r1); pk.y = pack2(r2, r3);
      *reinterpret_cast<uint2*>(out + (long)tok * D_ + col) = pk;
    }
}

extern "C" void kernel_launch(void* const* d_in, const int* in_sizes, int n_in,
                              void* d_out, int out_size, void* d_ws, size_t ws_size,
                              hipStream_t stream) {
  const u16* x = (const u16*)d_in[0];
  const u16* wqkv = (const u16*)d_in[1];
  const u16* wo = (const u16*)d_in[2];
  const u16* wg = (const u16*)d_in[4];
  const u16* wu = (const u16*)d_in[5];
  const u16* wd = (const u16*)d_in[6];
  u16* out = (u16*)d_out;
  char* ws = (char*)d_ws;
  u16* Qb = (u16*)(ws);
  u16* Kb = (u16*)(ws + (size_t)2 * 1024 * 1024);
  u16* Vb = (u16*)(ws + (size_t)4 * 1024 * 1024);
  u16* ob = (u16*)(ws + (size_t)6 * 1024 * 1024);
  float* bb = (float*)(ws + (size_t)8 * 1024 * 1024);
  u16* hb = (u16*)(ws + (size_t)12 * 1024 * 1024);
  k1_ln_gemm<<<256, K1_THREADS, 0, stream>>>(x, wqkv, wg, wu, Qb, Kb, Vb, hb STAMP_ARG((unsigned long long*)(ws + (size_t)64 * 1024 * 1024)));
  k2_attn_down<<<NB_DOWN + NB_ATTN, 256, 0, stream>>>(Qb, Kb, Vb, ob, hb, wd, bb STAMP_ARG((unsigned long long*)(ws + (size_t)96 * 1024 * 1024)));
  k3_wo_final<<<256, 256, 0, stream>>>(ob, wo, bb, x, out);
}
```

```cpp
#include <hip/hip_runtime.h>
#include <hip/hip_bf16.h>

typedef unsigned short u16;
typedef short bf16x8 __attribute__((ext_vector_type(8)));
typedef short bf16x4 __attribute__((ext_vector_type(4)));
typedef float f32x4 __attribute__((ext_vector_type(4)));
typedef float f32x16 __attribute__((ext_vector_type(16)));
typedef __attribute__((address_space(3))) bf16x4* lds_bf16x4_ptr;

#define DEV __device__ __forceinline__

constexpr int S_ = 2048, D_ = 512, NQKV = 1536, DF = 1792, H_ = 32, DH = 16;

DEV float bf2f(u16 v) { return __uint_as_float(((unsigned)v) << 16); }
typedef __bf16 bf16x2_n __attribute__((ext_vector_type(2)));
typedef float f32x2_n __attribute__((ext_vector_type(2)));
DEV unsigned pack2(float lo, float hi) {
  f32x2_n f = {lo, hi};
  bf16x2_n r = __builtin_convertvector(f, bf16x2_n);
  return *reinterpret_cast<unsigned*>(&r);
}
DEV bf16x4 tr_read(const char* lds_base, unsigned off) {
  return __builtin_amdgcn_ds_read_tr16_b64_v4i16((lds_bf16x4_ptr)(lds_base + off));
}
DEV void glds16(const void* sbase, unsigned voff, unsigned lds_dst) {
  unsigned keep;
  asm volatile("s_mov_b32 %0, m0\n\ts_mov_b32 m0, %3\n\ts_nop 0\n\tglobal_load_lds_dwordx4 %1, %2\n\ts_mov_b32 m0, %0"
               : "=&s"(keep) : "v"(voff), "s"(sbase), "s"(lds_dst) : "memory");
}
DEV unsigned lds_addr(const void* p) { return (unsigned)(size_t)p; }
DEV bf16x8 cat8(bf16x4 a, bf16x4 b) {
  bf16x8 r;
  r[0] = a[0]; r[1] = a[1]; r[2] = a[2]; r[3] = a[3];
  r[4] = b[0]; r[5] = b[1]; r[6] = b[2]; r[7] = b[3];
  return r;
}
DEV float dot2bf(unsigned a, unsigned b, float c) {
  typedef short s16x2 __attribute__((ext_vector_type(2)));
  return __builtin_amdgcn_fdot2_f32_bf16(*reinterpret_cast<s16x2*>(&a), *reinterpret_cast<s16x2*>(&b), c, false);
}
DEV float max3(float a, float b, float c) {
  float r; asm("v_max3_f32 %0, %1, %2, %3" : "=v"(r) : "v"(a), "v"(b), "v"(c)); return r;
}
DEV float gelu_tanh(float g) {
  const float c0 = 2.f * 0.7978845608028654f * 1.4426950408889634f, c1 = 0.044715f;
  const float y = c0 * g * fmaf(c1 * g, g, 1.f);
  return g * __builtin_amdgcn_rcpf(1.f + __builtin_amdgcn_exp2f(-y));
}
DEV float silu(float v) { return v * __builtin_amdgcn_rcpf(1.f + __builtin_amdgcn_exp2f(-1.4426950408889634f * v)); }

constexpr int K1_THREADS = 512;
constexpr int K1_A_STAGE = 128 * 64 * 2;
constexpr int K1_B_STAGE = 64 * 320 * 2;
constexpr int K1_LDS = 2 * K1_A_STAGE + 3 * K1_B_STAGE;

__global__ __launch_bounds__(K1_THREADS, 2) void k1_ln_gemm(
    const u16* __restrict__ x, const u16* __restrict__ wqkv, const u16* __restrict__ wg, const u16* __restrict__ wu,
    u16* __restrict__ Qo, u16* __restrict__ Ko, u16* __restrict__ Vto, u16* __restrict__ ho) {
  __shared__ __attribute__((aligned(16))) char lds[K1_LDS];
  char* As = lds;
  char* Bs = lds + 2 * K1_A_STAGE;

  const int tid = threadIdx.x, lane = tid & 63, wid = tid >> 6;
  const int wm = wid >> 2, wn = wid & 3;
  const int bid = blockIdx.x;
  const int xcd = bid & 7, idx = bid >> 3;
  const int mt = (xcd >> 1) * 4 + (idx & 3);
  const int nt = (xcd & 1) * 8 + (idx >> 2);
  const int m0 = mt * 128;
  const bool mixed = nt < 14;

  const u16* bsrc[5]; int bld[5];
  if (mixed) {
    bsrc[0] = wqkv + nt * 64;           bld[0] = NQKV;
    bsrc[1] = wg + nt * 128;            bld[1] = DF;
    bsrc[2] = wg + nt * 128 + 64;       bld[2] = DF;
    bsrc[3] = wu + nt * 128;            bld[3] = DF;
    bsrc[4] = wu + nt * 128 + 64;       bld[4] = DF;
  } else {
#pragma unroll
    for (int j = 0; j < 5; ++j) { bsrc[j] = wqkv + 896 + (nt - 14) * 320 + j * 64; bld[j] = NQKV; }
  }
  const int wave_lds = __builtin_amdgcn_readfirstlane(wid) * 1024;
  unsigned loff_q, loff_f;
  {
    const int k = tid >> 3, pc = tid & 7;
    const int fk = ((k >> 1) & 1) | (((k >> 3) & 1) << 1);
    const int lc = pc ^ (fk << 1);
    loff_q = (unsigned)(k * NQKV + lc * 8) * 2u;
    loff_f = (unsigned)(k * DF + lc * 8) * 2u;
  }
  const unsigned bs_lds = lds_addr(Bs) + wave_lds;
  auto stageB = [&](int stage, int s) {
#pragma unroll
    for (int j = 0; j < 5; ++j) {
      const unsigned lo = (bld[j] == NQKV) ? loff_q : loff_f;
      glds16(bsrc[j] + (long)s * 64 * bld[j], lo, bs_lds + stage * K1_B_STAGE + j * 8192);
    }
  };

  uint4 xr[8][2];
  {
    const int r = tid >> 2, j = tid & 3;
    const u16* xp = x + (long)(m0 + r) * D_ + j * 16;
#pragma unroll
    for (int s = 0; s < 8; ++s) {
      xr[s][0] = *reinterpret_cast<const uint4*>(xp + s * 64);
      xr[s][1] = *reinterpret_cast<const uint4*>(xp + s * 64 + 8);
    }
    asm volatile("" ::: "memory");
    stageB(0, 0);
    stageB(1, 1);
    float sum = 0.f;
#pragma unroll
    for (int s = 0; s < 8; ++s)
#pragma unroll
      for (int q = 0; q < 2; ++q) {
        const unsigned* w = reinterpret_cast<const unsigned*>(&xr[s][q]);
#pragma unroll
        for (int e = 0; e < 4; ++e) sum = dot2bf(w[e], 0x3F803F80u, sum);
      }
    sum += __shfl_xor(sum, 1); sum += __shfl_xor(sum, 2);
    const float mean = sum * (1.f / D_);
    f32x2_n vs2 = {0.f, 0.f};
    const f32x2_n mean2 = {mean, mean};
#pragma unroll
    for (int s = 0; s < 8; ++s)
#pragma unroll
      for (int q = 0; q < 2; ++q) {
        const unsigned* w = reinterpret_cast<const unsigned*>(&xr[s][q]);
#pragma unroll
        for (int e = 0; e < 4; ++e) {
          f32x2_n d = {__uint_as_float(w[e] << 16), __uint_as_float(w[e] & 0xffff0000u)};
          d = d - mean2;
          vs2 = d * d + vs2;
        }
      }
    float vs = vs2[0] + vs2[1];
    vs += __shfl_xor(vs, 1); vs += __shfl_xor(vs, 2);
    const float rstd = 1.f / sqrtf(vs * (1.f / D_) + 1e-5f);
    const float nb = -mean * rstd;
#pragma unroll
    for (int s = 0; s < 8; ++s)
#pragma unroll
      for (int q = 0; q < 2; ++q) {
        unsigned* w = reinterpret_cast<unsigned*>(&xr[s][q]);
#pragma unroll
        for (int e = 0; e < 4; ++e) {
          w[e] = pack2(fmaf(__uint_as_float(w[e] << 16), rstd, nb), fmaf(__uint_as_float(w[e] & 0xffff0000u), rstd, nb));
        }
        asm volatile("" : "+v"(w[0]), "+v"(w[1]), "+v"(w[2]), "+v"(w[3]));
      }
  }
  auto stageA = [&](int stage, int s) {
    const int r = tid >> 2, j = tid & 3;
    char* dst = As + stage * K1_A_STAGE + r * 128;
    *reinterpret_cast<uint4*>(dst + (((2 * j) ^ (r & 7)) << 4)) = xr[s][0];
    *reinterpret_cast<uint4*>(dst + (((2 * j + 1) ^ (r & 7)) << 4)) = xr[s][1];
  };
  stageA(0, 0);

  const int arow = wm * 64 + (lane & 15);
  const int g = lane >> 4, q = (lane & 15) >> 2, p = lane & 3;
  const int fk = (q >> 1) | ((g & 1) << 1);
  unsigned boff[5];
#pragma unroll
  for (int f = 0; f < 5; ++f) {
    int cf;
    if (mixed) cf = (f == 0) ? 16 * wn : (f <= 2 ? 64 + 32 * wn + 16 * (f - 1) : 192 + 32 * wn + 16 * (f - 3));
    else cf = 80 * wn + 16 * f;
    boff[f] = (cf >> 6) * 8192 + (8 * g + q) * 128 + ((((cf & 63) >> 4) ^ fk) << 5) + p * 8;
  }

  f32x4 acc[4][5];
#pragma unroll
  for (int m = 0; m < 4; ++m)
#pragma unroll
    for (int f = 0; f < 5; ++f) acc[m][f] = f32x4{0.f, 0.f, 0.f, 0.f};

#pragma unroll
  for (int s = 0; s < 8; ++s) {
    if (s < 7) asm volatile("s_waitcnt vmcnt(5) lgkmcnt(0)" ::: "memory");
    else asm volatile("s_waitcnt vmcnt(0) lgkmcnt(0)" ::: "memory");
    __builtin_amdgcn_s_barrier();
    asm volatile("" ::: "memory");
    if (s + 1 < 8) stageA((s + 1) & 1, s + 1);
    if (s + 2 < 8) stageB((s + 2) % 3, s + 2);
    const char* Ab = As + (s & 1) * K1_A_STAGE;
    const char* Bb = Bs + (s % 3) * K1_B_STAGE;
#pragma unroll
    for (int ks = 0; ks < 2; ++ks) {
      bf16x8 af[4], bfr[5];
#pragma unroll
      for (int m = 0; m < 4; ++m) {
        const int r = arow + 16 * m;
        af[m] = *reinterpret_cast<const bf16x8*>(Ab + r * 128 + ((((lane >> 4) + 4 * ks) ^ (r & 7)) << 4));
      }
#pragma unroll
      for (int f = 0; f < 5; ++f) {
        bf16x4 lo = tr_read(Bb, boff[f] + (32 * ks) * 128);
        bf16x4 hi = tr_read(Bb, boff[f] + (32 * ks + 4) * 128);
        bfr[f] = cat8(lo, hi);
      }
#pragma unroll
      for (int m = 0; m < 4; ++m)
#pragma unroll
        for (int f = 0; f < 5; ++f)
          acc[m][f] = __builtin_amdgcn_mfma_f32_16x16x32_bf16(bfr[f], af[m], acc[m][f], 0, 0, 0);
    }
  }
  __syncthreads();

  int tz = threadIdx.x; asm volatile("" : "+v"(tz));
  const int ln = tz & 63, wd_ = __builtin_amdgcn_readfirstlane(tz >> 6), wm2 = wd_ >> 2, wn2 = wd_ & 3;
  const int n4 = (ln >> 4) * 4;
  constexpr int QK_IMG = 4096, H_IMG = QK_IMG + 20 * 4096, H_LD = 272;
  auto put_qkv = [&](int col, int slot, int tokl, const f32x4& v) {
    const int which = col >> 9, h = (col & 511) >> 4;
    uint2 pk; pk.x = pack2(v[0], v[1]); pk.y = pack2(v[2], v[3]);
    if (which < 2) {
      *reinterpret_cast<uint2*>(lds + QK_IMG + slot * 4096 + tokl * 32 + n4 * 2) = pk;
    } else {
      char* tp = lds + wd_ * 512;
      *reinterpret_cast<uint2*>(tp + (ln & 15) * 32 + n4 * 2) = pk;
      asm volatile("s_waitcnt lgkmcnt(0)" ::: "memory");
      bf16x4 tv = tr_read(tp, ((ln >> 4) * 4 + ((ln & 15) >> 2)) * 32 + (ln & 3) * 8);
      asm volatile("" ::: "memory");
      const int tk0 = m0 + tokl - (ln & 15), g4 = ln >> 4;
      u16* dst = Vto + h * (S_ * 16) + (unsigned)(tk0 * 16 + (g4 & 1) * 128 + (ln & 15) * 8 + (g4 >> 1) * 4);
      *reinterpret_cast<bf16x4*>(dst) = tv;
    }
  };
#pragma unroll
  for (int m = 0; m < 4; ++m) {
    const int tokl = wm2 * 64 + 16 * m + (ln & 15);
    if (mixed) {
      put_qkv(nt * 64 + 16 * wn2, wn2, tokl, acc[m][0]);
#pragma unroll
      for (int f = 0; f < 2; ++f) {
        const f32x4 gg = acc[m][1 + f], uu = acc[m][3 + f];
        float hv[4];
#pragma unroll
        for (int j = 0; j < 4; ++j) hv[j] = gelu_tanh(gg[j]) * uu[j];
        uint2 pk; pk.x = pack2(hv[0], hv[1]); pk.y = pack2(hv[2], hv[3]);
        *reinterpret_cast<uint2*>(lds + H_IMG + tokl * H_LD + (32 * wn2 + 16 * f + n4) * 2) = pk;
      }
    } else {
#pragma unroll
      for (int f = 0; f < 5; ++f) put_qkv(896 + (nt - 14) * 320 + 80 * wn2 + 16 * f, wn2 * 5 + f, tokl, acc[m][f]);
    }
  }
  __syncthreads();
  {
    const int nslots = mixed ? 4 : 20;
    const int colbase = mixed ? nt * 64 : 896 + (nt - 14) * 320;
    for (int sl = tz >> 8; sl < nslots; sl += 2) {
      const int col = colbase + 16 * sl;
      const int which = col >> 9, h = (col & 511) >> 4;
      if (which < 2) {
        const uint4 v = *reinterpret_cast<const uint4*>(lds + QK_IMG + sl * 4096 + (tz & 255) * 16);
        *reinterpret_cast<uint4*>((which == 0 ? Qo : Ko) + h * (S_ * 16) + m0 * 16 + (tz & 255) * 8) = v;
      }
    }
    if (mixed) {
#pragma unroll
      for (int i = 0; i < 4; ++i) {
        const int row = (tz >> 4) + 32 * i, ch = tz & 15;
        const uint4 v = *reinterpret_cast<const uint4*>(lds + H_IMG + row * H_LD + ch * 16);
        *reinterpret_cast<uint4*>(ho + nt * 128 + (unsigned)((m0 + row) * DF + ch * 8)) = v;
      }
    }
  }
}

constexpr int G_STAGE = 8192;
template <int KTOT>
DEV void gemm64(const u16* __restrict__ A, int lda, const u16* __restrict__ B, int ldb, int m0, int n0,
                char* lds, f32x4 (&acc)[2][2]) {
  char* As = lds;
  char* Bs = lds + 4 * G_STAGE;
  const int tid = threadIdx.x, lane = tid & 63, wid = tid >> 6;
  const int wm = wid >> 1, wn = wid & 1;
  unsigned aoff, boff;
  {
    const int r = tid >> 3, pc = tid & 7;
    aoff = (unsigned)(r * lda + ((pc ^ (r & 7)) * 8)) * 2u;
    const int fk = ((r >> 1) & 1) | (((r >> 3) & 1) << 1);
    boff = (unsigned)(r * ldb + ((pc ^ (fk << 1)) * 8)) * 2u;
  }
  const u16* abase = A + (long)m0 * lda;
  const u16* bbase = B + n0;
  const int wave_lds = __builtin_amdgcn_readfirstlane(wid) * 1024;
  const unsigned as_lds = lds_addr(As) + wave_lds, bs_lds = lds_addr(Bs) + wave_lds;
  auto stage = [&](int st, int s) {
    glds16(abase + s * 64, aoff, as_lds + st * G_STAGE);
    glds16(abase + (long)32 * lda + s * 64, aoff, as_lds + st * G_STAGE + 4096);
    glds16(bbase + (long)s * 64 * ldb, boff, bs_lds + st * G_STAGE);
    glds16(bbase + (long)(s * 64 + 32) * ldb, boff, bs_lds + st * G_STAGE + 4096);
  };
  const int g = lane >> 4, q = (lane & 15) >> 2, p = lane & 3;
  const int fk = (q >> 1) | ((g & 1) << 1);
  constexpr int NS = KTOT / 64;
  stage(0, 0); stage(1, 1); stage(2, 2);
  for (int s = 0; s < NS; ++s) {
    if (s + 2 < NS) asm volatile("s_waitcnt vmcnt(8)" ::: "memory");
    else if (s + 1 < NS) asm volatile("s_waitcnt vmcnt(4)" ::: "memory");
    else asm volatile("s_waitcnt vmcnt(0)" ::: "memory");
    __builtin_amdgcn_s_barrier();
    asm volatile("" ::: "memory");
    if (s + 3 < NS) stage((s + 3) & 3, s + 3);
    const char* Ab = As + (s & 3) * G_STAGE;
    const char* Bb = Bs + (s & 3) * G_STAGE;
#pragma unroll
    for (int ks = 0; ks < 2; ++ks) {
      bf16x8 af[2], bfr[2];
#pragma unroll
      for (int m = 0; m < 2; ++m) {
        const int r = wm * 32 + 16 * m + (lane & 15);
        af[m] = *reinterpret_cast<const bf16x8*>(Ab + r * 128 + ((((lane >> 4) + 4 * ks) ^ (r & 7)) << 4));
      }
#pragma unroll
      for (int n = 0; n < 2; ++n) {
        const unsigned off = (8 * g + q + 32 * ks) * 128 + ((((wn * 2 + n)) ^ fk) << 5) + p * 8;
        bfr[n] = cat8(tr_read(Bb, off), tr_read(Bb, off + 4 * 128));
      }
#pragma unroll
      for (int m = 0; m < 2; ++m)
#pragma unroll
        for (int n = 0; n < 2; ++n)
          acc[m][n] = __builtin_amdgcn_mfma_f32_16x16x32_bf16(bfr[n], af[m], acc[m][n], 0, 0, 0);
    }
  }
}

constexpr int NB_DOWN = (S_ / 64) * (D_ / 64);
constexpr int NB_ATTN = H_ * (S_ / 32) / 4;

DEV void attn_wave(const u16* __restrict__ Q, const u16* __restrict__ Kk, const u16* __restrict__ Vt,
                   u16* __restrict__ o, int h, int q0, int lane) {
  const int r = lane & 31, hh = lane >> 5;
  const bf16x8 qf = *reinterpret_cast<const bf16x8*>(Q + ((long)h * S_ + q0 + r) * 16 + 8 * hh);
  bf16x8 kf[9];
#pragma unroll
  for (int t = 0; t < 9; ++t) {
    const int key0 = max(q0 - 256 + 32 * t, 0);
    kf[t] = *reinterpret_cast<const bf16x8*>(Kk + ((long)h * S_ + key0 + r) * 16 + 8 * hh);
  }
  bf16x8 vf[9][2];
  {
    const short one = (short)0x3F80;
    bf16x8 ones8;
#pragma unroll
    for (int i = 0; i < 8; ++i) ones8[i] = one;
    const bool isone = (r >= 16);
    const u16* vbase = Vt + (long)h * (S_ * 16) + hh * 128 + (r & 15) * 8;
#pragma unroll
    for (int t = 0; t < 9; ++t) {
      const int key0 = max(q0 - 256 + 32 * t, 0);
#pragma unroll
      for (int ss = 0; ss < 2; ++ss) {
        const bf16x8 v = *reinterpret_cast<const bf16x8*>(vbase + (key0 + 16 * ss) * 16);
        vf[t][ss] = isone ? ones8 : v;
      }
    }
  }
  f32x16 st[9];
  const float NEG = -3.0e38f;
#pragma unroll
  for (int t = 0; t < 9; ++t) {
    f32x16 z;
#pragma unroll
    for (int i = 0; i < 16; ++i) z[i] = 0.f;
    st[t] = __builtin_amdgcn_mfma_f32_32x32x16_bf16(kf[t], qf, z, 0, 0, 0);
  }
#pragma unroll
  for (int i = 0; i < 16; ++i) {
    const int kl = (i & 3) + 8 * (i >> 2) + 4 * hh;
    if (kl <= r) st[0][i] = NEG;
    if (kl > r) st[8][i] = NEG;
  }
  if (q0 < 256) {
#pragma unroll
    for (int t = 0; t < 8; ++t)
      if (q0 - 256 + 32 * t < 0) {
#pragma unroll
        for (int i = 0; i < 16; ++i) st[t][i] = NEG;
      }
  }
  float mx = NEG;
#pragma unroll
  for (int t = 0; t < 9; ++t)
#pragma unroll
    for (int i = 0; i < 16; i += 2) mx = max3(mx, st[t][i], st[t][i + 1]);
  mx = fmaxf(mx, __shfl_xor(mx, 32));
  const float c = 0.25f * 1.4426950408889634f;
  const float mc = mx * c;
  f32x16 oacc;
#pragma unroll
  for (int i = 0; i < 16; ++i) oacc[i] = 0.f;
#pragma unroll
  for (int t = 0; t < 9; ++t) {
#pragma unroll
    for (int ss = 0; ss < 2; ++ss) {
      bf16x8 pf;
#pragma unroll
      for (int e = 0; e < 4; ++e) {
        const float p0 = __builtin_amdgcn_exp2f(st[t][8 * ss + 2 * e] * c - mc);
        const float p1 = __builtin_amdgcn_exp2f(st[t][8 * ss + 2 * e + 1] * c - mc);
        const unsigned w = pack2(p0, p1);
        pf[2 * e] = (short)(w & 0xffff); pf[2 * e + 1] = (short)(w >> 16);
      }
      oacc = __builtin_amdgcn_mfma_f32_32x32x16_bf16(vf[t][ss], pf, oacc, 0, 0, 0);
    }
  }
  const float inv = 1.f / oacc[8];
  u16* op = o + (long)(q0 + r) * D_ + h * 16 + 4 * hh;
  uint2 a, b;
  a.x = pack2(oacc[0] * inv, oacc[1] * inv); a.y = pack2(oacc[2] * inv, oacc[3] * inv);
  b.x = pack2(oacc[4] * inv, oacc[5] * inv); b.y = pack2(oacc[6] * inv, oacc[7] * inv);
  *reinterpret_cast<uint2*>(op) = a;
  *reinterpret_cast<uint2*>(op + 8) = b;
}

__global__ __launch_bounds__(256, 2) void k2_attn_down(
    const u16* __restrict__ Q, const u16* __restrict__ Kk, const u16* __restrict__ Vt, u16* __restrict__ o,
    const u16* __restrict__ hbuf, const u16* __restrict__ wd, float* __restrict__ bout) {
  __shared__ __attribute__((aligned(16))) char lds[8 * G_STAGE];
  const int bid = blockIdx.x, tid = threadIdx.x, lane = tid & 63, wid = tid >> 6;
  if (bid < NB_DOWN) {
    const int mt = (bid & 7) * 4 + ((bid >> 3) & 3), nt = bid >> 5;
    f32x4 acc[2][2];
#pragma unroll
    for (int m = 0; m < 2; ++m)
#pragma unroll
      for (int n = 0; n < 2; ++n) acc[m][n] = f32x4{0.f, 0.f, 0.f, 0.f};
    gemm64<DF>(hbuf, DF, wd, D_, mt * 64, nt * 64, lds, acc);
    const int wm = wid >> 1, wn = wid & 1;
#pragma unroll
    for (int m = 0; m < 2; ++m)
#pragma unroll
      for (int n = 0; n < 2; ++n) {
        const int tok = mt * 64 + wm * 32 + 16 * m + (lane & 15);
        const int col = nt * 64 + wn * 32 + 16 * n + (lane >> 4) * 4;
        *reinterpret_cast<f32x4*>(bout + (long)tok * D_ + col) = acc[m][n];
      }
  } else {
    const int task = (bid - NB_DOWN) * 4 + __builtin_amdgcn_readfirstlane(wid);
    const int h = task >> 6, qt = task & 63;
    attn_wave(Q, Kk, Vt, o, h, qt * 32, lane);
  }
}

__global__ __launch_bounds__(256, 2) void k3_wo_final(
    const u16* __restrict__ o, const u16* __restrict__ wo, const float* __restrict__ bout,
    const u16* __restrict__ x, u16* __restrict__ out) {
  __shared__ __attribute__((aligned(16))) char lds[8 * G_STAGE];
  const int bid = blockIdx.x, tid = threadIdx.x, lane = tid & 63, wid = tid >> 6;
  const int mt = (bid & 7) * 4 + ((bid >> 3) & 3), nt = bid >> 5;
  f32x4 acc[2][2];
#pragma unroll
  for (int m = 0; m < 2; ++m)
#pragma unroll
    for (int n = 0; n < 2; ++n) acc[m][n] = f32x4{0.f, 0.f, 0.f, 0.f};
  const int wm = wid >> 1, wn = wid & 1;
  f32x4 bv[2][2]; uint2 xv[2][2];
#pragma unroll
  for (int m = 0; m < 2; ++m)
#pragma unroll
    for (int n = 0; n < 2; ++n) {
      const int tok = mt * 64 + wm * 32 + 16 * m + (lane & 15);
      const int col = nt * 64 + wn * 32 + 16 * n + (lane >> 4) * 4;
      bv[m][n] = *reinterpret_cast<const f32x4*>(bout + (long)tok * D_ + col);
      xv[m][n] = *reinterpret_cast<const uint2*>(x + (long)tok * D_ + col);
    }
  gemm64<D_>(o, D_, wo, D_, mt * 64, nt * 64, lds, acc);
#pragma unroll
  for (int m = 0; m < 2; ++m)
#pragma unroll
    for (int n = 0; n < 2; ++n) {
      const int tok = mt * 64 + wm * 32 + 16 * m + (lane & 15);
      const int col = nt * 64 + wn * 32 + 16 * n + (lane >> 4) * 4;
      float r0 = __uint_as_float(xv[m][n].x << 16) + acc[m][n][0] * silu(bv[m][n][0]);
      float r1 = __uint_as_float(xv[m][n].x & 0xffff0000u) + acc[m][n][1] * silu(bv[m][n][1]);
      float r2 = __uint_as_float(xv[m][n].y << 16) + acc[m][n][2] * silu(bv[m][n][2]);
      float r3 = __uint_as_float(xv[m][n].y & 0xffff0000u) + acc[m][n][3] * silu(bv[m][n][3]);
      uint2 pk; pk.x = pack2(r0, r1); pk.y = pack2(r2, r3);
      *reinterpret_cast<uint2*>(out + (long)tok * D_ + col) = pk;
    }
}

extern "C" void kernel_launch(void* const* d_in, const int* in_sizes, int n_in,
                              void* d_out, int out_size, void* d_ws, size_t ws_size,
                              hipStream_t stream) {
  const u16* x = (const u16*)d_in[0];
  const u16* wqkv = (const u16*)d_in[1];
  const u16* wo = (const u16*)d_in[2];
  const u16* wg = (const u16*)d_in[4];
  const u16* wu = (const u16*)d_in[5];
  const u16* wd = (const u16*)d_in[6];
  u16* out = (u16*)d_out;
  char* ws = (char*)d_ws;
  u16* Qb = (u16*)(ws);
  u16* Kb = (u16*)(ws + (size_t)2 * 1024 * 1024);
  u16* Vb = (u16*)(ws + (size_t)4 * 1024 * 1024);
  u16* ob = (u16*)(ws + (size_t)6 * 1024 * 1024);
  float* bb = (float*)(ws + (size_t)8 * 1024 * 1024);
  u16* hb = (u16*)(ws + (size_t)12 * 1024 * 1024);
  k1_ln_gemm<<<256, K1_THREADS, 0, stream>>>(x, wqkv, wg, wu, Qb, Kb, Vb, hb);
  k2_attn_down<<<NB_DOWN + NB_ATTN, 256, 0, stream>>>(Qb, Kb, Vb, ob, hb, wd, bb);
  k3_wo_final<<<256, 256, 0, stream>>>(ob, wo, bb, x, out);
}
```
